```python
import torch

def _softmax(x, axis=-1):
    return torch.softmax(x, dim=axis)
def _gelu(x):
    return torch.nn.functional.gelu(x, approximate="tanh")
def _silu(x):
    return torch.nn.functional.silu(x)
def _sigmoid(x):
    return torch.sigmoid(x)
def _layernorm(x, axis=-1, eps=1e-5):
    m = x.mean(dim=axis, keepdim=True)
    v = x.var(dim=axis, keepdim=True, unbiased=False)
    return (x - m) / torch.sqrt(v + eps)
def _scatter_add(tgt, idx, src, axis=0):
    return tgt.clone().index_add_(axis, idx, src)
def _segment_sum(src, seg, n):
    out = torch.zeros((n,) + src.shape[1:], dtype=src.dtype)
    return out.index_add_(0, seg, src)
def _pad(x, pw):
    flat = tuple(v for pair in reversed(pw) for v in pair)
    return torch.nn.functional.pad(x, flat)

kernel_name = 'sequential_preLN__spectral_gfnet__moe_arch_mod__prompt'

def setup_inputs():

    _w = (lambda a: (lambda t: t.to(torch.bfloat16) if t.is_floating_point() else t)(torch.as_tensor(a)))
    def _A_setup():
        B, S, D = 1, 2048, 576
        Df = D // 2 + 1
        return {
            "x": _w((lambda *s: torch.randn(*s, dtype=torch.bfloat16) * 0.05)(B, S, D)),
            "w_r": _w((lambda *s: torch.randn(*s, dtype=torch.bfloat16) * 0.4)(S, Df) * 0.02),
            "w_i": _w((lambda *s: torch.randn(*s, dtype=torch.bfloat16) * 0.4)(S, Df) * 0.02),
        }
    _A_in = _A_setup()

    def _B_setup():
        B, S, D, F, C = 1, 2048, 576, 4608, 2
        return {
            "x": _w((lambda *s: torch.randn(*s, dtype=torch.bfloat16) * 0.05)(B, S, D)),
            "w_r": _w((lambda *s: torch.randn(*s, dtype=torch.bfloat16) * 0.05)(D)),
            "w_gate": _w((lambda *s: torch.randn(*s, dtype=torch.bfloat16) * 0.05)(D, F)),
            "w_up": _w((lambda *s: torch.randn(*s, dtype=torch.bfloat16) * 0.05)(D, F)),
            "w_down": _w((lambda *s: torch.randn(*s, dtype=torch.bfloat16) * 0.05)(F, D)),
        }
    _B_in = _B_setup()

    return {
        "A_x": _A_in["x"],
        "A_w_r": _A_in["w_r"],
        "A_w_i": _A_in["w_i"],
        "B_x": _B_in["x"],
        "B_w_r": _B_in["w_r"],
        "B_w_gate": _B_in["w_gate"],
        "B_w_up": _B_in["w_up"],
        "B_w_down": _B_in["w_down"],
    }

def reference(A_x, A_w_r, A_w_i, B_x, B_w_r, B_w_gate, B_w_up, B_w_down):
    def _A_ref(x, w_r, w_i):
        B, S, D = x.shape
        X = (lambda x, axes=(-2,-1): torch.fft.rfft2(x.float(), dim=axes))(x, axes=(-2, -1))
        W = w_r + 1j * w_i
        X = X * W
        y = (lambda x, s, axes=(-2,-1): torch.fft.irfft2(x, s=s, dim=axes).to(torch.bfloat16))(X, (S, D), axes=(-2, -1))
        return y

    def _B_ref(x, w_r, w_gate, w_up, w_down):
        B, S, D = x.shape
        C = 2
        r = x @ w_r
        idx = torch.argsort(-r, axis=-1)[:, :C]
        idx_e = (lambda x, n, axis: torch.repeat_interleave(x, n, dim=axis))(idx.reshape(B, C, 1), D, axis=2)
        x_sel = (lambda x, i, axis: torch.take_along_dim(x, i, dim=axis))(x, idx_e, axis=1)
        g = _silu(x_sel @ w_gate)
        u = x_sel @ w_up
        y = (g * u) @ w_down
        r_sel = _sigmoid((lambda x, i, axis: torch.take_along_dim(x, i, dim=axis))(r, idx, axis=1))
        y = y * r_sel.reshape(B, C, 1)
        b_off = (torch.arange(B) * S).reshape(B, 1)
        flat_idx = (idx + b_off).reshape(B * C)
        zero = (x * 0.0).reshape(B * S, D)
        upd = _scatter_add(zero, flat_idx, y.reshape(B * C, D), axis=0)
        return x + upd.reshape(B, S, D)

    x = A_x
    h = x + _A_ref(_layernorm(x, axis=-1), A_w_r, A_w_i)
    h2 = h + _B_ref(_layernorm(h, axis=-1), B_w_r, B_w_gate, B_w_up, B_w_down)
    return h2
```

```cpp
#include <hip/hip_runtime.h>

typedef unsigned u32;
typedef unsigned long long u64;
typedef unsigned short u16;
typedef u32 v4u __attribute__((ext_vector_type(4)));
typedef u32 v3u __attribute__((ext_vector_type(3)));
typedef u32 v2u __attribute__((ext_vector_type(2)));
typedef float f4 __attribute__((ext_vector_type(4)));
typedef float f2 __attribute__((ext_vector_type(2)));
typedef _Float16 h8 __attribute__((ext_vector_type(8)));
typedef _Float16 h2 __attribute__((ext_vector_type(2)));
typedef __bf16 b8 __attribute__((ext_vector_type(8)));
typedef __bf16 b2 __attribute__((ext_vector_type(2)));
typedef short s4 __attribute__((ext_vector_type(4)));
#define LAS __attribute__((address_space(3)))
#define GAS __attribute__((address_space(1)))
#define DI __device__ __forceinline__
#define BC __builtin_bit_cast
template <class T> struct MA { typedef T __attribute__((may_alias)) type; };

enum { T_HC = 0, T_TW3 = 8, T_HB = 12, T_R8I = 14, T_RW = 16, T_G = 18, T_LT = 23 };

constexpr u32 WS_EPOCH = 0, WS_HINT = 16384, WS_E3 = 65536, WS_E1 = 0x20000, PITCH = 12416, PITCH2 = 28800;
constexpr u32 WS_E2 = WS_E1 + 256 * PITCH, WS_CAND = WS_E2 + 128 * PITCH2, WS_E4 = WS_CAND + 128 * 6144, WS_END = WS_E4 + 128 * 6144;
constexpr u32 WS_DIAG = WS_END;
constexpr u32 L_TAB = 0, L_ROW = 29696, ROWP = 1216, L_XI = L_ROW + 17 * ROWP, L_RST = L_XI + 17 * 1792, L_S1 = L_RST + 24896,
              L_WR = L_S1 + 24624, L_KEY = L_WR + 24576 + 64, L_END = L_KEY + 128;
constexpr u32 L_PC = L_RST, L_S2 = L_RST, L_Y = L_XI, L_CS = L_RST;
constexpr u32 F_GU = 0, F_UP = 46080, F_DN = 92160 + 160, F_PART = F_DN + 46080, F_AV = F_PART + 5120, F_OS = F_AV + 256, F_TOP = F_OS + 5120, F_END = F_TOP + 16;
constexpr u32 LDS_BYTES = L_END > F_END ? L_END : F_END;
static_assert(LDS_BYTES <= 163840, "LDS");
constexpr u32 OOB = 0x80000000u, FWB = 2048 * 578;
constexpr int SPIN = 1 << 15;
#define DLY1 4
#define DLY2 4
#define DLY3 56
#define DLY4 30
#define DLY5 12
#define FDLY 10
#define WDLY 0
#define CODEB 26112
#define PREW 1
#define FAUX 0
#define NP3 1
#define PD3 5
#define NP4 1
#define PD4 5
#define OAUX 16
#define TLY3 110
#define GTOUCH 0
#define PRIO 2

DI u32 pkh(float a, float b) { h2 v = {(_Float16)a, (_Float16)b}; return BC(u32, v); }
DI u32 pkb(float a, float b) { b2 v = {(__bf16)a, (__bf16)b}; return BC(u32, v); }
DI u32 pkbo(f2 a) { u32 v = pkb(a.x, a.y); asm("" : "+v"(v)); return v; }
DI float blo(u32 x) { return BC(float, x << 16); }
DI float bhi(u32 x) { return BC(float, x & 0xFFFF0000u); }
DI f2 unp(u32 x) { return f2{blo(x), bhi(x)}; }
DI float rbf(float a) { return blo(pkb(a, a)); }
DI float dot2(u32 a, u32 b, float c) { return __builtin_amdgcn_fdot2_f32_bf16(BC(b2, a), BC(b2, b), c, false); }
DI f4 mh(v4u a, v4u b, f4 c) { return __builtin_amdgcn_mfma_f32_16x16x32_f16(BC(h8, a), BC(h8, b), c, 0, 0, 0); }
DI f4 mb(v4u a, v4u b, f4 c) { return __builtin_amdgcn_mfma_f32_16x16x32_bf16(BC(b8, a), BC(b8, b), c, 0, 0, 0); }
template <int C, int RM> DI float dppf(float v) { return BC(float, __builtin_amdgcn_update_dpp(0, BC(int, v), C, RM, 0xf, true)); }
template <int C, int RM> DI u32 dppu(u32 v) { return (u32)__builtin_amdgcn_update_dpp(0, (int)v, C, RM, 0xf, true); }
DI float rl(float v, int i) { return BC(float, __builtin_amdgcn_readlane(BC(int, v), i)); }
DI float wsum(float v) {
    v += dppf<0x111, 0xf>(v); v += dppf<0x112, 0xf>(v); v += dppf<0x114, 0xf>(v); v += dppf<0x118, 0xf>(v);
    v += dppf<0x142, 0xa>(v); v += dppf<0x143, 0xc>(v);
    return rl(v, 63);
}
DI float swadd32(float a, float b) {
    const auto q = __builtin_amdgcn_permlane32_swap(BC(u32, a), BC(u32, b), false, false);
    const u32 q0 = q[0], q1 = q[1];
    return BC(float, q0) + BC(float, q1);
}
DI float rowsum(float v) { v += dppf<0x111, 0xf>(v); v += dppf<0x112, 0xf>(v); v += dppf<0x114, 0xf>(v); v += dppf<0x118, 0xf>(v); return v; }
DI float wsum2v(float a, float b) { float v = rowsum(swadd32(a, b)); v += dppf<0x142, 0xa>(v); return v; }
DI float wsum4(float a, float b, float c, float d) {
    const auto q = __builtin_amdgcn_permlane16_swap(BC(u32, swadd32(a, c)), BC(u32, swadd32(b, d)), false, false);
    const u32 q0 = q[0], q1 = q[1];
    return rowsum(BC(float, q0) + BC(float, q1));
}
DI u32 h2b(u32 v, float k) { const h2 t = BC(h2, v); return pkb((float)t.x * k, (float)t.y * k); }
DI u32 umax(u32 a, u32 b) { return a > b ? a : b; }
DI u32 wmax(u32 v) {
    v = umax(v, dppu<0x111, 0xf>(v)); v = umax(v, dppu<0x112, 0xf>(v)); v = umax(v, dppu<0x114, 0xf>(v)); v = umax(v, dppu<0x118, 0xf>(v));
    v = umax(v, dppu<0x142, 0xa>(v)); v = umax(v, dppu<0x143, 0xc>(v));
    return (u32)__builtin_amdgcn_readlane((int)v, 63);
}
template <bool BF> DI v4u rot(f4 xr, f4 xi, f4 cr, f4 ci) {
    const f4 re = xr * cr - xi * ci, im = xr * ci + xi * cr;
    if (BF) return v4u{pkb(re[0], re[1]), pkb(re[2], re[3]), pkb(im[0], im[1]), pkb(im[2], im[3])};
    return v4u{pkh(re[0], re[1]), pkh(re[2], re[3]), pkh(im[0], im[1]), pkh(im[2], im[3])};
}
#define WS() asm volatile("" ::: "memory")
#define DRAIN() asm volatile("s_waitcnt vmcnt(0)" ::: "memory")
#define PIN(x) asm volatile("" : "+v"(x) :: "memory")
DI u64 uni(const void* p) { const u64 v = (u64)p; return (u64)(u32)__builtin_amdgcn_readfirstlane((u32)v) | ((u64)(u32)__builtin_amdgcn_readfirstlane((u32)(v >> 32)) << 32); }
DI void dma16(const void* sbase, u32 voff, u32 ldst) {
    u32 keep;
    asm volatile("s_mov_b32 %0, m0\n\ts_mov_b32 m0, %3\n\ts_nop 3\n\tglobal_load_lds_dwordx4 %1, %2\n\ts_mov_b32 m0, %0"
                 : "=&s"(keep) : "v"(voff), "s"(uni(sbase)), "s"(__builtin_amdgcn_readfirstlane(ldst)) : "memory");
}
DI void dma16m(u64 sbase, u32 voff, u32 ldst, u64 mask) {
    u32 keep;
    asm volatile("s_mov_b32 %0, m0\n\ts_mov_b32 m0, %3\n\ts_mov_b64 exec, %4\n\ts_nop 2\n\tglobal_load_lds_dwordx4 %1, %2\n\ts_mov_b64 exec, -1\n\ts_mov_b32 m0, %0"
                 : "=&s"(keep) : "v"(voff), "s"(sbase), "s"(ldst), "s"(mask) : "memory");
}
DI void touch(const void* sbase, u32 voff, u32 ldst) {
    u32 keep;
    asm volatile("s_mov_b32 %0, m0\n\ts_mov_b32 m0, %3\n\ts_nop 3\n\tglobal_load_lds_dword %1, %2\n\ts_mov_b32 m0, %0"
                 : "=&s"(keep) : "v"(voff), "s"(uni(sbase)), "s"(__builtin_amdgcn_readfirstlane(ldst)) : "memory");
}
DI float cosr(float v) { return __builtin_amdgcn_cosf(v); }
DI float sinr(float v) { return __builtin_amdgcn_sinf(v); }
#define STAMP(k)
#define SDUMP(lo, hi, ww)
#define WDUMP()
#define BAR() asm volatile("s_waitcnt lgkmcnt(0)\n\ts_barrier" ::: "memory")

__global__ void __launch_bounds__(768, 3)
fused_block(const u16* __restrict__ x, const u16* fwr, const u16* fwi, const u16* __restrict__ rw,
            const u16* __restrict__ wg, const u16* __restrict__ wu, const u16* __restrict__ wd, u16* __restrict__ out, unsigned char* ws) {
    __shared__ __attribute__((aligned(128))) unsigned char lds_[LDS_BYTES];
    LAS unsigned char* lds = (LAS unsigned char*)lds_;
    const u32 lb = (u32)(size_t)lds;
#define LR(T, off) (*(LAS typename MA<T>::type*)(lds + (off)))
#define TABR(i) LR(v4u, L_TAB + (i) * 1024 + l * 16)
#define TABF(i) LR(f4, L_TAB + (i) * 1024 + l * 16)
#define TRR(off) BC(v2u, __builtin_amdgcn_ds_read_tr16_b64_v4i16((LAS s4*)(lds + (off))))
    const int t = threadIdx.x, l = t & 63, r = l & 15, g = l >> 4;
    const int w = __builtin_amdgcn_readfirstlane(t >> 6);
    const int m = (blockIdx.x & 7) * 16 + (blockIdx.x >> 4);
#define rs __builtin_amdgcn_make_buffer_rsrc(ws, 0, WS_END, 0x00020000)
#define ro __builtin_amdgcn_make_buffer_rsrc(out, 0, 2048 * 1152, 0x00020000)
#define ST16(v, off) __builtin_amdgcn_raw_buffer_store_b128((v), rs, (off), 0, 16)
#define LD16(off) __builtin_amdgcn_raw_buffer_load_b128(rs, (off), 0, 16)
#define ST16R(rr, v, off, so, ax) do { const v4u v_ = (v); __builtin_amdgcn_raw_buffer_store_b128(v_, rr, (off), (so), ax); asm volatile("s_nop 1" :: "v"(v_) : "memory"); } while (0)
#define ST16S(v, off, so) ST16R(rs, v, off, so, 16)
#define LD16S(off, so) __builtin_amdgcn_raw_buffer_load_b128(rs, (off), (so), 16)
    const f4 z4 = {0.f, 0.f, 0.f, 0.f};
    int rt1 = 0, rt2 = 0, rt3 = 0, rt4 = 0, rt5 = 0;
    STAMP(0);
    u64 pc0;
    asm volatile("s_getpc_b64 %0" : "=s"(pc0));
#define TOP2(K1, K2, first) { \
        v4u ea = LD16(WS_E3 + l * 128), eb = LD16(WS_E3 + 8192 + l * 128); \
        first; \
        _Pragma("nounroll") for (rt3 = 0; rt3 < SPIN; ++rt3) { \
            if (__all((((ea.w ^ tag) | (eb.w ^ tag)) | ((ea.z ^ tag) | (eb.z ^ tag))) == 0)) break; \
            WS(); ea = LD16(WS_E3 + l * 128); eb = LD16(WS_E3 + 8192 + l * 128); } \
        const u32 b1 = umax(ea.x, eb.x), b2 = umax(ea.x < eb.x ? ea.x : eb.x, umax(ea.y, eb.y)); \
        K1 = wmax(b1); K2 = wmax(b1 == K1 ? b2 : b1); }

    if (__builtin_expect((blockIdx.x >> 3) & 1, 0)) {
        if (w >= 10) return;
        asm volatile("" : "+s"(ws), "+s"(fwr), "+s"(fwi));
        if (CODEB && w == 8) {
#pragma unroll
            for (int i = 0; i < (CODEB + 8191) / 8192; ++i) { const u32 o = l * 128 + i * 8192; touch((const void*)pc0, o < CODEB ? o : 0u, lb + F_OS + 1024); }
        }
        if (PREW && w == 9) {
            touch(ws, l * (WS_END / 64 & ~127u), lb + F_OS + 1280); touch(out, l * 36864, lb + F_OS + 1280);
            if (PREW > 1) { touch(ws, WS_CAND + l * 24576, lb + F_OS + 1280); touch(ws, l * 2048, lb + F_OS + 1280); }
        }
        if (w < 4) {
            const u32 c = 64 * (w & 1) + l, rr = c / 6, off = ((m >> 4) + 8 * (16 * (m & 15) + rr)) * 578 + 128 * (c - 6 * rr);
            if (c < 96 && off < FWB) touch(w & 2 ? fwi : fwr, off & ~3u, lb + F_OS + 256 * w);
        }
        if (w < 8) {
            __builtin_amdgcn_s_sleep(FDLY);
            if (GTOUCH) {
#pragma unroll 1
                for (int i = w; i < 12; i += 8) {
                    const u32 c = 64 * (i >> 1) + l, r9 = c / 9;
                    if (c < 324) touch((i & 1) ? wu : wg, (36 * (m & 15) + r9) * 9216 + 1152 * (m >> 4) + 128 * (c - 9 * r9), lb + F_OS + 1536 + 256 * w);
                }
            }
            const u32 k6 = (l * 52429u) >> 19, p6 = l - 10 * k6, kk = (l * 52429u) >> 18, p = l - 5 * kk, c0 = (72 * m) & ~15;
            if (l < 60) {
#pragma unroll 1
                for (int i = 0; i < 6; ++i) dma16(wd, (36 * m + 6 * i + k6) * 1152 + 144 * w + 16 * ((w == 7 && p6 == 9) ? 8 : p6), lb + F_DN + 5760 * w + 960 * i);
                if (GTOUCH) { DRAIN(); __builtin_amdgcn_s_sleep(GTOUCH); }
                int ii = m % 6;
#pragma unroll 1
                for (int i = 0; i < 6; ++i) {
                    dma16(wg, (72 * w + 12 * ii + kk) * 9216 + c0 + 16 * p, lb + F_GU + 5760 * w + 960 * ii);
                    dma16(wu, (72 * w + 12 * ii + kk) * 9216 + c0 + 16 * p, lb + F_UP + 5760 * w + 960 * ii);
                    ii = ii == 5 ? 0 : ii + 1;
                }
            }
        } else if (w == 9) {
            LR(u32, F_AV + l * 4) = 0;
        }
        if (w == 8) {
            const u32 tag = __builtin_amdgcn_readfirstlane(__hip_atomic_load((GAS u32*)(ws + WS_EPOCH), __ATOMIC_RELAXED, __HIP_MEMORY_SCOPE_AGENT)) + 0x9E3779B9u;
            u32 ts = 0;
            _Pragma("nounroll") for (int s = 0; s < SPIN; ++s) {
                WS();
                __builtin_amdgcn_s_sleep(8);
                const v4u hn = LD16(WS_HINT + m * 128);
                ts = __builtin_amdgcn_readfirstlane(hn.x);
                if (__all(hn.w == tag)) break;
            }
            STAMP(27);
            _Pragma("nounroll") for (int s = 0; s < 256 && (u32)__builtin_amdgcn_s_memrealtime() - ts < TLY3; ++s) __builtin_amdgcn_s_sleep(1);
            STAMP(28);
            u32 K1, K2;
            {
                v4u pa_[NP3], pb_[NP3];
#pragma unroll
                for (int i = 0; i < NP3; ++i) {
                    pa_[i] = LD16(WS_E3 + l * 128); pb_[i] = LD16(WS_E3 + 8192 + l * 128);
                    WS();
                    if (i + 1 < NP3) __builtin_amdgcn_s_sleep(PD3);
                    WS();
                }
                v4u ea = pa_[0], eb = pb_[0];
                bool ok = __all((((ea.w ^ tag) | (eb.w ^ tag)) | ((ea.z ^ tag) | (eb.z ^ tag))) == 0);
#pragma unroll
                for (int i = 1; i < NP3; ++i) {
                    if (!ok) { ea = pa_[i]; eb = pb_[i]; ok = __all((((ea.w ^ tag) | (eb.w ^ tag)) | ((ea.z ^ tag) | (eb.z ^ tag))) == 0); rt3 = i; }
                }
                _Pragma("nounroll") for (int s = 0; !ok && s < SPIN; ++s) {
                    WS(); ea = LD16(WS_E3 + l * 128); eb = LD16(WS_E3 + 8192 + l * 128);
                    ok = __all((((ea.w ^ tag) | (eb.w ^ tag)) | ((ea.z ^ tag) | (eb.z ^ tag))) == 0); rt3 = NP3 + s;
                }
                const u32 b1 = umax(ea.x, eb.x), b2 = umax(ea.x < eb.x ? ea.x : eb.x, umax(ea.y, eb.y));
                K1 = wmax(b1); K2 = wmax(b1 == K1 ? b2 : b1);
            }
            LR(v4u, F_TOP) = v4u{K1, K2, tag, 0u};
            BAR();
            SDUMP(27, 28, 8); SDUMP(23, 23, 8);
            return;
        }
        DRAIN();
        STAMP(16);
        if (w == 9) {
            const int ci = 3 * m + ((l & 3) < 3 ? (l & 3) : 0), c24 = ci / 24, rtok = c24 & 1, rc3 = (c24 >> 1) * 24 + ci - 24 * c24;
            u32 eo = WS_E4 + (l >> 2) * 6144 + ci * 16;
            PIN(eo);
            BAR();
            const v4u kt = LR(v4u, F_TOP);
            const u32 tag = __builtin_amdgcn_readfirstlane(kt.z);
            const u32 K = rtok ? kt.y : kt.x, rowk = 2047 - ((K & 0xFFF) >> 1);
            const u32 fo = WS_CAND + ((rowk & 255) >> 1) * 6144 + (K & 1) * 3072 + (rc3 >> 1) * 16;
            v4u fl = LD16(fo), fh = LD16(fo + 1536);
            const u32 srt = K >> 16;
            const u32 rb_ = (srt & 0x8000u) ? (srt & 0x7FFFu) : (srt ? (~srt & 0xFFFFu) : 0x7FC0u);
            float sig = rbf(__builtin_amdgcn_rcpf(1.f + __expf(-BC(float, rb_ << 16))));
            _Pragma("nounroll") for (rt4 = 0; rt4 < SPIN; ++rt4) {
                if (__all(((fl.w ^ tag) | (fh.w ^ tag)) == 0)) break;
                WS();
                fl = LD16(fo); fh = LD16(fo + 1536);
            }
            const bool od = rc3 & 1;
            float lv[3] = {od ? bhi(fl.y) : blo(fl.x), od ? blo(fl.z) : bhi(fl.x), od ? bhi(fl.z) : blo(fl.y)};
            float hv[3] = {od ? bhi(fh.y) : blo(fh.x), od ? blo(fh.z) : bhi(fh.x), od ? bhi(fh.z) : blo(fh.y)};
            u32 oo = l == 0 ? rowk * 1152 + rc3 * 6 : OOB;
            PIN(oo); PIN(sig); PIN(lv[0]); PIN(lv[1]); PIN(lv[2]); PIN(hv[0]); PIN(hv[1]); PIN(hv[2]);
            BAR(); BAR();
            __builtin_amdgcn_s_sleep(DLY4);
            STAMP(29);
            v4u v[8], vv[NP4][8];
#pragma unroll
            for (int k = 0; k < NP4; ++k) {
#pragma unroll
                for (int i = 0; i < 8; ++i) vv[k][i] = __builtin_amdgcn_raw_buffer_load_b128(rs, eo, i * 16 * 6144, 16);
                WS();
                if (k + 1 < NP4) __builtin_amdgcn_s_sleep(PD4);
                WS();
            }
            bool ok = false;
#pragma unroll
            for (int k = 0; k < NP4; ++k) {
                if (!ok) {
                    u32 mm = 0;
#pragma unroll
                    for (int i = 0; i < 8; ++i) { v[i] = vv[k][i]; mm |= v[i].w ^ tag; }
                    ok = __all(mm == 0); rt5 = k;
                }
            }
            _Pragma("nounroll") for (int s = 0; !ok && s < SPIN; ++s) {
                WS();
                u32 mm = 0;
#pragma unroll
                for (int i = 0; i < 8; ++i) v[i] = __builtin_amdgcn_raw_buffer_load_b128(rs, eo, i * 16 * 6144, 16);
#pragma unroll
                for (int i = 0; i < 8; ++i) mm |= v[i].w ^ tag;
                ok = __all(mm == 0); rt5 = NP4 + s;
            }
            if (m == 0 && l == 0) __hip_atomic_store((GAS u32*)(ws + WS_EPOCH), tag, __ATOMIC_RELAXED, __HIP_MEMORY_SCOPE_AGENT);
            float ov[3];
            f4 s = ((BC(f4, v[0]) + BC(f4, v[1])) + (BC(f4, v[2]) + BC(f4, v[3]))) + ((BC(f4, v[4]) + BC(f4, v[5])) + (BC(f4, v[6]) + BC(f4, v[7])));
#pragma unroll
            for (int i = 0; i < 3; ++i) {
                float a = s[i];
                a += dppf<0x124, 0xf>(a); a += dppf<0x128, 0xf>(a);
                auto q = __builtin_amdgcn_permlane16_swap(BC(u32, a), BC(u32, a), false, false);
                u32 q0 = q[0], q1 = q[1]; a = BC(float, q0) + BC(float, q1);
                q = __builtin_amdgcn_permlane32_swap(BC(u32, a), BC(u32, a), false, false);
                q0 = q[0]; q1 = q[1]; a = BC(float, q0) + BC(float, q1);
                ov[i] = hv[i] + rbf(lv[i] + rbf(rbf(a) * sig));
            }
            {
                float e[9];
#pragma unroll
                for (int i = 0; i < 3; ++i) { e[i] = ov[i]; e[3 + i] = dppf<0x55, 0xf>(ov[i]); e[6 + i] = dppf<0xAA, 0xf>(ov[i]); }
                const int o_ = m & 1;
                const v4u q4 = o_ ? v4u{pkb(e[1], e[2]), pkb(e[3], e[4]), pkb(e[5], e[6]), pkb(e[7], e[8])} : v4u{pkb(e[0], e[1]), pkb(e[2], e[3]), pkb(e[4], e[5]), pkb(e[6], e[7])};
                __builtin_amdgcn_raw_buffer_store_b128(q4, ro, oo + 2 * o_, 0, FAUX);
                __builtin_amdgcn_raw_buffer_store_b16((u16)pkb(o_ ? e[0] : e[8], 0.f), ro, oo + (o_ ? 0 : 16), 0, FAUX);
            }
            STAMP(15);
            SDUMP(15, 15, 9); SDUMP(25, 25, 9); SDUMP(29, 29, 9);
            return;
        }
        v4u gu[15], dn[10];
        {
            const u32 ta = F_GU + (72 * w + 6 * g + (r >> 2)) * 80 + (l & 3) * 8;
#pragma unroll
            for (int s = 0; s < 3; ++s) {
                const u32 a0 = ta + s * 24 * 80;
                const u32 aa[5] = {a0, a0 + 32, a0 + ((l & 2) ? F_UP - 16 : 64), a0 + F_UP + 16, a0 + F_UP + 48};
#pragma unroll
                for (int tl = 0; tl < 5; ++tl) { const v2u lo = TRR(aa[tl]), hi = TRR(aa[tl] + 320); gu[5 * s + tl] = v4u{lo.x, lo.y, hi.x, 0u}; }
            }
            const int f0 = 8 * g + (r >> 2);
            const u32 d0 = F_DN + 5760 * w + f0 * 160 + (l & 3) * 8, d1 = F_DN + 5760 * w + (32 + (r >> 2)) * 160 + (l & 3) * 8;
#pragma unroll
            for (int tl = 0; tl < 5; ++tl) {
                const v2u lo = TRR(d0 + 32 * tl), hi = TRR(d0 + 4 * 160 + 32 * tl), l2 = TRR(d1 + 32 * tl);
                dn[tl] = v4u{lo.x, lo.y, hi.x, hi.y}; dn[5 + tl] = v4u{l2.x, l2.y, l2.x, l2.y};
            }
        }
        const int la = t < 72 ? t : 71, tok = la >= 36, ff = la - 36 * tok, lo_ = l < 48 ? l : 0;
        u32 pa = F_PART + (80 * tok + ff + ((36 * m) & 7)) * 32, pw = F_PART + (r * 8 + w) * 4, avw = F_AV + tok * 128 + ff * 2, avr = F_AV + (r & 1) * 128 + g * 16;
        u32 osw = F_OS + w * 640 + (r & 1) * 320 + g * 16, osr = F_OS + w * 640 + lo_ * 12 + (lo_ >= 24 ? 32 : 0);
        u32 e4o = l < 48 ? WS_E4 + m * 6144 + w * 768 + l * 16 : OOB, xo = w * 192 + g * 16;
#pragma unroll
        for (int i = 0; i < 15; ++i) PIN(gu[i]);
#pragma unroll
        for (int i = 0; i < 10; ++i) PIN(dn[i]);
        PIN(pa); PIN(pw); PIN(avw); PIN(avr); PIN(osw); PIN(osr); PIN(e4o); PIN(xo);
        STAMP(11);
        BAR();
        const v4u kt = LR(v4u, F_TOP);
        const u32 tag = __builtin_amdgcn_readfirstlane(kt.z);
        {
            STAMP(12);
            const u32 K = (r & 1) ? kt.y : kt.x;
            xo += WS_CAND + (((2047 - ((K & 0xFFF) >> 1)) & 255) >> 1) * 6144 + (K & 1) * 3072;
            v4u a0 = LD16(xo), a1 = LD16(xo + 64), a2 = LD16(xo + 128);
            _Pragma("nounroll") for (rt4 = 0; rt4 < SPIN; ++rt4) {
                if (__all(((a0.w ^ tag) | (a1.w ^ tag) | (a2.w ^ tag)) == 0)) break;
                WS();
                a0 = LD16(xo); a1 = LD16(xo + 64); a2 = LD16(xo + 128);
            }
            STAMP(26);
            a0.w = 0; a1.w = 0; a2.w = 0;
            f4 acc[5];
#pragma unroll
            for (int tl = 0; tl < 5; ++tl) acc[tl] = mb(a2, gu[10 + tl], mb(a1, gu[5 + tl], mb(a0, gu[tl], z4)));
            if (g == 0) {
#pragma unroll
                for (int tl = 0; tl < 5; ++tl) { LR(float, pw + tl * 512) = acc[tl][0]; LR(float, pw + tl * 512 + 2560) = acc[tl][1]; }
            }
        }
        BAR();
        STAMP(13);
        if (w < 2) {
            const f4 g0 = LR(f4, pa) + LR(f4, pa + 16), u0 = LR(f4, pa + 40 * 32) + LR(f4, pa + 40 * 32 + 16);
            const float gs = rbf((g0[0] + g0[1]) + (g0[2] + g0[3])), us = rbf((u0[0] + u0[1]) + (u0[2] + u0[3]));
            const float sg = rbf(gs * __builtin_amdgcn_rcpf(1.f + __expf(-gs)));
            LR(u16, avw) = (u16)pkb(sg * us, 0.f);
        }
        BAR();
        {
            const v4u av0 = LR(v4u, avr), av1 = LR(v4u, avr + 64);
            f4 o[5];
#pragma unroll
            for (int tl = 0; tl < 5; ++tl) o[tl] = mb(dn[5 + tl], av1, mb(dn[tl], av0, z4));
            if (r < 2) {
#pragma unroll
                for (int tl = 0; tl < 5; ++tl) LR(f4, osw + 64 * tl) = o[tl];
            }
            WS();
            const v4u v = {LR(u32, osr), LR(u32, osr + 4), LR(u32, osr + 8), tag};
            ST16(v, e4o);
            STAMP(14);
            SDUMP(11, 14, 0); SDUMP(16, 16, 0); SDUMP(26, 26, 0);
        }
        return;
    }
    const bool xtra = (m & 15) == 15;
    const int row = 2 * m + 256 * (w & 7);
    v4u xa4[2]; v2u xb2[2];
    if (w < 8) {
        const GAS unsigned char* xr = (const GAS unsigned char*)x + row * 1152 + (l < 48 ? l : 47) * 24;
        xa4[0] = *(const GAS v4u*)xr; xb2[0] = *(const GAS v2u*)(xr + 16); xa4[1] = *(const GAS v4u*)(xr + 1152); xb2[1] = *(const GAS v2u*)(xr + 1168);
    }
    const float fr_ = (float)r, fg = (float)g;
    u32 l16 = l * 16 + lb;
    PIN(l16);
#define LQ(T, off) (*(LAS typename MA<T>::type*)(size_t)(off))
#undef TABR
#undef TABF
#define TABR(i) LQ(v4u, l16 + L_TAB + (i) * 1024)
#define TABF(i) LQ(f4, l16 + L_TAB + (i) * 1024)
#define TRQ(a) BC(v2u, __builtin_amdgcn_ds_read_tr16_b64_v4i16((LAS s4*)(size_t)(a)))
    {
        float c[4], s[4];
        const u32 pc = l16 + L_PC;
        if (w < 8) {
            const float ds = BC(float, (w & 6) == 2 ? 0x3AE38E39u : 0x3D2AAAABu), dofs = BC(float, w < 6 ? 0u : 0x3F2AAAABu);
            const float gs = BC(float, w < 2 ? 0x41000000u : 0x40800000u), bo = BC(float, (w & 1) ? (w < 2 ? 0x40800000u : 0x41800000u) : 0u);
            const float d = fr_ * ds + dofs, b0 = gs * fg + bo;
#pragma unroll
            for (int j = 0; j < 4; ++j) { const float a = (b0 + (float)j) * d; c[j] = cosr(a); s[j] = sinr(a); }
            if (w < 2) {
                const u32 m3 = g < 3 ? ~0u : 0u;
                LQ(v2u, pc + 8 * w) = v2u{pkb(c[0], c[1]) & m3, pkb(c[2], c[3]) & m3};
                LQ(v2u, pc + 1024 + 8 * w) = v2u{pkb(-s[0], -s[1]) & m3, pkb(-s[2], -s[3]) & m3};
            } else if (w < 4) {
                LQ(f4, pc + 2048 * (w - 1)) = f4{c[0], c[1], c[2], c[3]};
                LQ(f4, pc + 2048 * (w - 1) + 1024) = f4{-s[0], -s[1], -s[2], -s[3]};
            } else {
                const u32 mk = ((w & 1) && g >= 2) ? 0u : ~0u, sx = (w >= 6 || r >= 12) ? 0x80008000u : 0u;
                const u32 c01 = pkh(c[0], c[1]) & mk, c23 = pkh(c[2], c[3]) & mk, s01 = pkh(s[0], s[1]) & mk, s23 = pkh(s[2], s[3]) & mk;
                LQ(v4u, pc + 2048 * (w - 1)) = v4u{c01, c23, s01, s23};
                LQ(v4u, pc + 2048 * (w - 1) + 1024) = v4u{s01 ^ sx ^ 0x80008000u, s23 ^ sx ^ 0x80008000u, c01 ^ sx, c23 ^ sx};
            }
        } else if (w < 10) {
            const float on = (g & 1) == (r >> 3) ? 0.25f : 0.f;
            const int r7 = r & 7, p0_ = (2 * m + (w & 1) + 1024 * (g >> 1)) * r7, dp = 256 * r7;
#pragma unroll
            for (int j = 0; j < 4; ++j) {
                const float ph = (float)((p0_ + j * dp) & 2047) * (1.f / 2048.f);
                c[j] = on * cosr(ph); s[j] = on * sinr(ph);
            }
            LQ(v4u, pc + 14336 + (w & 1) * 2048) = v4u{pkh(c[0], s[0]), pkh(c[1], s[1]), pkh(c[2], s[2]), pkh(c[3], s[3])};
            LQ(v4u, pc + 15360 + (w & 1) * 2048) = v4u{pkh(-s[0], c[0]), pkh(-s[1], c[1]), pkh(-s[2], c[2]), pkh(-s[3], c[3])};
        } else if (w == 10) {
            const int rho = 12 * (g & 1) + 3 * (r >> 2) + (r & 3);
            const bool sp = (r & 3) == 3, rf = rho > 12;
            TABR(T_LT) = v4u{(u32)(lb + L_XI + (g >> 1) * 7168 + 4 * (sp ? 12 : (rf ? (24 - rho) * 28 + 23 : rho * 28))), (u32)(sp ? 0 : (rf ? -4 : 4)),
                             (u32)(lb + L_RST + ((r & 7) * 8 + (((r >> 3) * 4 + g) ^ (r & 7))) * 16), (u32)((l >> 3) * 1536 + (l & 7) * 16)};
        } else {
            TABR(T_LT + 1) = v4u{(u32)(lb + L_ROW + (8 * (g < 3 ? g : 0) + (r >> 2)) * 48 + (l & 3) * 8), (u32)(lb + L_ROW + (l < 49 ? l : 49) * 24),
                                 (u32)(lb + L_XI + (r * 28 + 4 * g) * 4), (u32)(lb + L_RST + ((l & 56) + ((l ^ (l >> 3)) & 7)) * 16)};
        }
    }
    STAMP(23);
    BAR();
    STAMP(25);
#define P1CONST \
        u32 pc = l16 + L_PC; \
        PIN(pc); \
        v4u fb0 = LQ(v4u, pc), fb1 = LQ(v4u, pc + 1024), fc[8]; \
        f4 tw[4]; \
        _Pragma("unroll") for (int i = 0; i < 4; ++i) tw[i] = LQ(f4, pc + (2 + i) * 1024); \
        _Pragma("unroll") for (int i = 0; i < 8; ++i) fc[i] = LQ(v4u, pc + (6 + i) * 1024); \
        const v4u ones = {0x3F803F80u, 0x3F803F80u, 0x3F803F80u, 0x3F803F80u}; \
        f4 sre = mb(ones, fb0, z4), sim = mb(ones, fb1, z4)
#define ROWDFT(A0_, A1_, nmean, rinv, xw_) { \
            const f4 cre = sre * nmean, cim = sim * nmean; \
            const v4u b0 = rot<false>(mb(A0_, fb0, cre), mb(A0_, fb1, cim), tw[0] * rinv, tw[1] * rinv); \
            const v4u b1 = rot<false>(mb(A1_, fb0, cre), mb(A1_, fb1, cim), tw[2] * rinv, tw[3] * rinv); \
            _Pragma("unroll") for (int mt = 0; mt < 2; ++mt) { \
                const f4 xr = mh(fc[4 * mt + 2], b1, mh(fc[4 * mt], b0, z4)); \
                const f4 xi = mh(fc[4 * mt + 3], b1, mh(fc[4 * mt + 1], b0, z4)); \
                if (mt == 0 || g < 2) LQ(v4u, (xw_) + 64 * mt) = v4u{pkh(xr[0], xi[0]), pkh(xr[1], xi[1]), pkh(xr[2], xi[2]), pkh(xr[3], xi[3])}; } }
#define SPEC2OP(dst, s0_, s1_) { \
            _Pragma("unroll") for (int i = 0; i < 4; ++i) { \
                dst[i] = h2b(g == 3 ? s0_[3 - i] : s0_[i], (i == 0 && l == 0) || (i == 3 && l == 48) ? 0.5f : 1.f); dst[4 + i] = h2b(s1_[3 - i], 1.f); } }
#define OP2LIN(src, a0_, a1_) { \
            if (r < 13) { _Pragma("unroll") for (int i = 0; i < 4; ++i) LQ(u32, (a0_) + 96 * i) = src[i]; \
                          if (g < 2) { _Pragma("unroll") for (int i = 0; i < 4; ++i) LQ(u32, (a1_) + 96 * i) = src[4 + i]; } } }
#define P2PREP(u0, du) \
        const v4u lt2 = TABR(T_LT + 2); \
        v4u g1a = TABR(T_G), g2a = TABR(T_G + 1), g3b = TABR(T_G + 2), g1b, g2b, g3a; \
        f4 cr = TABF(T_G + 3), cn = TABF(T_G + 4), fr[2], fi[2]; \
        P2BAR; \
        { const u32 a = lt2.z + 2 * ((m >> 4) & 1) + 2 * (u0), a3 = a + ((m == 127 && l == 63) ? 8 : 0); \
          if ((du) == 1) { _Pragma("unroll") for (int i = 0; i < 4; ++i) { \
              const u32 ai = (i == 3 ? a3 : a) + 768 * i, vr = LQ(u32, ai), vi = LQ(u32, ai + 12288); \
              fr[0][i] = blo(vr); fr[1][i] = bhi(vr); fi[0][i] = blo(vi); fi[1][i] = bhi(vi); } } \
          else { _Pragma("unroll") for (int u = 0; u < 2; ++u) _Pragma("unroll") for (int i = 0; i < 4; ++i) { \
              const u32 ai = (i == 3 ? a3 : a) + 768 * i + 2 * (du) * u; \
              fr[u][i] = BC(float, (u32)LQ(u16, ai) << 16); fi[u][i] = BC(float, (u32)LQ(u16, ai + 12288) << 16); } } } \
        _Pragma("unroll") for (int j = 0; j < 4; ++j) g1b[j] = __builtin_amdgcn_alignbit(g1a[j], g1a[j], 16) ^ 0x8000u; \
        g2b = v4u{g2a.z ^ 0x80008000u, g2a.w ^ 0x80008000u, g2a.x, g2a.y}; \
        g3a = v4u{g3b.z, g3b.w, g3b.x ^ 0x80008000u, g3b.y ^ 0x80008000u}; \
        f4 ci = -cn; \
        u32 s1r = lt2.x + (u0) * 1296; \
        PIN(g1a); PIN(g1b); PIN(g2a); PIN(g2b); PIN(g3a); PIN(g3b); PIN(cr); PIN(ci); PIN(cn); PIN(fr[0]); PIN(fi[0]); PIN(fr[1]); PIN(fi[1]); \
        auto unit = [&](v4u a, f4 wr_, f4 wi_, f4& xr, f4& xi) { \
            v4u f = rot<false>(mh(a, g1a, z4), mh(a, g1b, z4), cr, ci); \
            f = rot<true>(mh(g2a, f, z4), mh(g2b, f, z4), wr_, wi_); \
            f = rot<true>(mb(f, g3a, z4), mb(f, g3b, z4), cr, cn); \
            xr = mb(g3a, f, z4); xi = mb(g3b, f, z4); }
#define P2BAR
    if (__builtin_expect(w >= 8, 0)) {
        float c[4], s[4];
        if (w == 10) {
            dma16m((u64)rw, 16 * l, lb + L_ROW + 16 * ROWP, ~0ull);
            dma16m((u64)rw, l < 8 ? 1024 + 16 * l : 0, lb + L_ROW + 16 * ROWP + 1024, 0x1FFull);
            const v4u lt1 = TABR(T_LT + 1);
            P1CONST;
            const int cc = r < 12 ? r : 12;
            const v4u lt3 = {r < 12 ? (64 * (g >> 1) + (r >= 6)) * PITCH2 + 96 * (g & 1) + 16 * (r >= 6 ? r - 6 : r) : OOB, (u32)(lb + L_Y + r * 1168 + 48 * g),
                             (u32)(lb + L_Y + (g < 3 ? cc + 96 * g : 216 - cc) * 4), (u32)(lb + L_Y + (24 - cc + 96 * (g & 1)) * 4)};
            TABR(T_LT + 3) = lt3;
#pragma unroll
            for (int i = 0; i < 8; ++i) PIN(fc[i]);
            PIN(tw[0]); PIN(tw[1]); PIN(tw[2]); PIN(tw[3]); PIN(sre); PIN(sim);
            STAMP(20);
            BAR();
            DRAIN();
            const u32 wl_ = lb + L_ROW + 16 * ROWP + (l < 48 ? l : 47) * 24, ta = lt1.x + 16 * ROWP;
            const v2u w0_ = LQ(v2u, wl_), w1_ = LQ(v2u, wl_ + 8), w2_ = LQ(v2u, wl_ + 16);
            const v2u lo0 = TRQ(ta), hi0 = TRQ(ta + 192), lo1 = TRQ(ta + 32), hi1 = TRQ(ta + 224);
            const u32 wq[6] = {w0_.x, w0_.y, w1_.x, w1_.y, w2_.x, w2_.y};
            float s1 = 0.f, s2 = 0.f;
#pragma unroll
            for (int i = 0; i < 6; ++i) { s1 = dot2(wq[i], 0x3F803F80u, s1); s2 = dot2(wq[i], wq[i], s2); }
            const float v2 = wsum2v(l < 48 ? s1 : 0.f, l < 48 ? s2 : 0.f) * (1.f / 576.f), mw = rl(v2, 31);
            const float rvw = 0.0625f * __builtin_amdgcn_rsqf(fmaxf(rl(v2, 63) - mw * mw, 0.f) + 1e-10f * rl(v2, 63) + 1e-30f);
            const v4u a0 = {lo0.x, lo0.y, hi0.x, hi0.y}, a1 = {lo1.x, lo1.y, hi1.x, hi1.y};
            ROWDFT(a0, a1, -mw, rvw, lt1.z + 16 * 1792);
            WS();
            const v4u q0 = LQ(v4u, lt1.z + 16 * 1792), q1 = LQ(v4u, lt1.z + 16 * 1792 + 80 - 16 * (g & 1) - 16 * g);
            u32 vw[8];
            SPEC2OP(vw, q0, q1);
            OP2LIN(vw, lt3.z - L_Y + L_TAB + T_RW * 1024, lt3.w - L_Y + L_TAB + T_RW * 1024);
            LR(f2, L_KEY + 104) = f2{mw, __builtin_amdgcn_rcpf(9.f * rvw)};
            STAMP(19);
            BAR();
            WDUMP();
            return;
        }
        if (w == 11) {
            const u32 ep = __hip_atomic_load((GAS u32*)(ws + WS_EPOCH), __ATOMIC_RELAXED, __HIP_MEMORY_SCOPE_AGENT);
#pragma unroll
            for (int j = 0; j < 4; ++j) { const float a = (4.f * fg + (float)j) * fr_ * (1.f / 16.f); c[j] = 0.25f * cosr(a); s[j] = 0.25f * sinr(a); }
            TABR(T_G) = v4u{pkh(c[0], s[0]), pkh(c[1], s[1]), pkh(c[2], s[2]), pkh(c[3], s[3])};
            TABR(T_G + 1) = v4u{pkh(c[0], c[1]), pkh(c[2], c[3]), pkh(s[0], s[1]), pkh(s[2], s[3])};
            TABR(T_G + 2) = v4u{pkb(s[0], s[1]), pkb(s[2], s[3]), pkb(c[0], c[1]), pkb(c[2], c[3])};
#pragma unroll
            for (int j = 0; j < 4; ++j) { const float a = (4.f * fg + (float)j) * fr_ * (1.f / 256.f); c[j] = cosr(a); s[j] = sinr(a); }
            TABF(T_G + 3) = f4{c[0], c[1], c[2], c[3]};
            TABF(T_G + 4) = f4{s[0], s[1], s[2], s[3]};
            TABR(T_LT + 2) = v4u{(u32)(lb + L_S1 + (r * 20 + 4 * g) * 4), (u32)(lb + L_S2 + (64 * g + r) * 16), (u32)(lb + L_WR + (64 * g + r) * 48), (u32)((64 * g + r) * 16)};
            LR(u32, L_KEY + 96) = ep + 0x9E3779B9u;
            STAMP(20);
            BAR();
            const u32 y0 = (24 * r + 4 * g) * 2;
            TABR(T_LT + 4) = v4u{lb + L_ROW + y0, lb + L_ROW + (r < 8 ? y0 + 768 : 1152 + (g & 1) * 8), (u32)(lb + L_ROW + (l < 48 ? l : 47) * 24), 0u};
#pragma unroll
            for (int o = 0; o < 2; ++o) {
                const float lb_ = fr_ + (float)(16 * o);
                const bool in24 = !o || r < 8;
#pragma unroll
                for (int j = 0; j < 4; ++j) { const float a = (4.f * fg + (float)j) * lb_ * (1.f / 576.f); c[j] = cosr(a); s[j] = sinr(a); }
                TABF(T_TW3 + 2 * o) = f4{c[0], c[1], c[2], c[3]} * (8.f / 9.f);
                TABF(T_TW3 + 2 * o + 1) = f4{s[0], s[1], s[2], s[3]} * (8.f / 9.f);
#pragma unroll
                for (int j = 0; j < 4; ++j) { const float a = (4.f * fg + (float)j) * lb_ * (1.f / 24.f); c[j] = cosr(a); s[j] = sinr(a); }
                const float w0 = !in24 ? 0.f : ((g == 0 || g == 3) ? 0.25f : 0.5f), w1 = (!in24 || g == 3) ? 0.f : 0.5f;
                TABR(T_HB + o) = v4u{pkb(w0 * c[0], w1 * c[1]), pkb(w1 * c[2], w1 * c[3]), pkb(-w0 * s[0], -w1 * s[1]), pkb(-w1 * s[2], -w1 * s[3])};
            }
            STAMP(19);
            BAR();
            WDUMP();
            return;
        }
        const int o1 = w & 1;
        {
            const u32 kk = (l * 43691u) >> 17, vo = kk * (4624 - 48) + 16 * l;
            const u64 sb = (u64)(o1 ? fwi : fwr) + (((m >> 4) * 578 + 36 * (m & 15)) & ~3u);
            __builtin_amdgcn_s_sleep(WDLY);
#pragma unroll 1
            for (int i = 0; i < 12; ++i) dma16m(sb + 21 * 4624 * i, vo, lb + L_WR + o1 * 12288 + i * 1008, 0x7FFFFFFFFFFFFFFFull);
            dma16m(sb + 21 * 4624 * 12, vo - ((m == 127 && l >= 9) ? 8 : 0), lb + L_WR + o1 * 12288 + 12 * 1008, 0xFFFull);
        }
        STAMP(20);
        BAR();
        const u32 tag = __builtin_amdgcn_readfirstlane(LR(u32, L_KEY + 96));
        {
            const float d24 = (fr_ + BC(float, o1 ? 0x41800000u : 0u)) * (1.f / 24.f);
            const bool in24 = !o1 || r < 8;
#pragma unroll
            for (int sx = 0; sx < 2; ++sx) {
                const float e0 = sx ? 23.f - 4.f * fg : (g < 3 ? 4.f * fg : 15.f), de = (sx || g == 3) ? -1.f : 1.f;
#pragma unroll
                for (int j = 0; j < 4; ++j) { const float a = (e0 + (float)j * de) * d24; c[j] = cosr(a); s[j] = sinr(a); }
                const float on = (in24 && (!sx || g < 2)) ? 0.25f : 0.f, sg = on * de;
                TABR(T_HC + 4 * sx + 2 * o1) = v4u{pkb(on * c[0], -sg * s[0]), pkb(on * c[1], -sg * s[1]), pkb(on * c[2], -sg * s[2]), pkb(on * c[3], -sg * s[3])};
                TABR(T_HC + 4 * sx + 2 * o1 + 1) = v4u{pkb(on * s[0], sg * c[0]), pkb(on * s[1], sg * c[1]), pkb(on * s[2], sg * c[2]), pkb(on * s[3], sg * c[3])};
            }
            if (o1) {
                const int dp = 2 * m + (g & 1) + 256 * (r & 7), p0_ = dp * 4 * (g >> 1);
                const float on = (g & 1) == (r >> 3) ? 0.25f : 0.f;
#pragma unroll
                for (int j = 0; j < 4; ++j) { const float ph = (float)((p0_ + j * dp) & 2047) * (1.f / 2048.f); c[j] = on * cosr(ph); s[j] = on * sinr(ph); }
                TABR(T_R8I) = v4u{pkb(c[0], -s[0]), pkb(c[1], -s[1]), pkb(c[2], -s[2]), pkb(c[3], -s[3])};
                TABR(T_R8I + 1) = v4u{pkb(s[0], c[0]), pkb(s[1], c[1]), pkb(s[2], c[2]), pkb(s[3], c[3])};
            }
        }
        DRAIN();
        STAMP(19);
        BAR();
        const int uh = o1 ? 17 : (((m >> 4) & 1) ? 0 : 16);
        P2PREP(uh, 18 - uh + 2 * o1);
        u32 s2c = lt2.y + (uh / 3) * 4144 + (uh % 3) * 4;
        PIN(s1r); PIN(s2c);
        STAMP(17);
        __builtin_amdgcn_s_setprio(3);
        BAR();
        f4 xr, xi, yr, yi;
        if (xtra && !o1) {
            const v4u a = LQ(v4u, s1r), b = LQ(v4u, lt2.x + 18 * 1296);
            unit(a, fr[0], fi[0], xr, xi); unit(b, fr[1], fi[1], yr, yi);
        } else {
            const v4u a = LQ(v4u, s1r);
            unit(a, fr[0], fi[0], xr, xi);
        }
#pragma unroll
        for (int i = 0; i < 4; ++i) LQ(u32, s2c + i * 256) = pkb(xr[i], xi[i]);
        STAMP(26);
        BAR();
        if (xtra && !o1) {
#pragma unroll
            for (int i = 0; i < 4; ++i) ST16S((v4u{pkb(yr[i], yi[i]), 0u, 0u, tag}), lt2.w + i * 256, WS_E2 + m * PITCH2 + 24576);
        }
        WDUMP();
        return;
    }
    u32 tag;
    float pst[8];
    v4u xs[4];
    const u32 rowo = w * ROWP;
    {
        const v4u lt1 = TABR(T_LT + 1), lt0 = TABR(T_LT);
        P1CONST;
        v4u r8[4];
#pragma unroll
        for (int i = 0; i < 4; ++i) r8[i] = LQ(v4u, pc + (14 + i) * 1024);
        u32 ta = lt1.x + rowo, zw = lt1.y + rowo, xw = lt1.z + w * 1792;
        u32 xa0 = lt0.x + lt0.y * w, xa2 = lt0.x + lt0.y * (8 + (w & 3)) + (w >> 2) * 14336, rsw = lt0.z + w * 1024, rsr = lt1.w + w * 1024, e1l = lt0.w;
        PIN(sre); PIN(sim); PIN(xa0); PIN(xa2); PIN(rsw); PIN(rsr); PIN(ta); PIN(zw); PIN(xw); PIN(r8[3]);
        STAMP(1);
        asm volatile("" : "+v"(xa4[0]), "+v"(xb2[0]), "+v"(xa4[1]), "+v"(xb2[1]) :: "memory");
        STAMP(2);
        __builtin_amdgcn_s_setprio(PRIO);
#pragma unroll
        for (int h = 0; h < 2; ++h) {
            LQ(v2u, zw + h * 8 * ROWP) = v2u{xa4[h].x, xa4[h].y}; LQ(v2u, zw + h * 8 * ROWP + 8) = v2u{xa4[h].z, xa4[h].w}; LQ(v2u, zw + h * 8 * ROWP + 16) = xb2[h];
        }
        WS();
        v4u a0[2], a1[2];
#pragma unroll
        for (int h = 0; h < 2; ++h) {
            const v2u lo0 = TRQ(ta + h * 8 * ROWP), hi0 = TRQ(ta + h * 8 * ROWP + 192), lo1 = TRQ(ta + h * 8 * ROWP + 32), hi1 = TRQ(ta + h * 8 * ROWP + 224);
            a0[h] = v4u{lo0.x, lo0.y, hi0.x, hi0.y}; a1[h] = v4u{lo1.x, lo1.y, hi1.x, hi1.y};
        }
        float st[4];
#pragma unroll
        for (int h = 0; h < 2; ++h) {
            const u32 xp[6] = {xa4[h].x, xa4[h].y, xa4[h].z, xa4[h].w, xb2[h].x, xb2[h].y};
            float s1 = 0.f, s2 = 0.f;
#pragma unroll
            for (int i = 0; i < 6; ++i) { s1 = dot2(xp[i], 0x3F803F80u, s1); s2 = dot2(xp[i], xp[i], s2); }
            st[2 * h] = l < 48 ? s1 : 0.f; st[2 * h + 1] = l < 48 ? s2 : 0.f;
        }
        const float vs = wsum4(st[0], st[1], st[2], st[3]) * (1.f / 576.f);
        const float m1 = dppf<0x142, 0xa>(vs);
        const float rv = 0.0625f * __builtin_amdgcn_rcpf(rbf(__builtin_amdgcn_sqrtf(rbf(rbf(fmaxf(vs - m1 * m1, 0.f)) + 1e-5f)))), mv = -rbf(m1);
#pragma unroll
        for (int h = 0; h < 2; ++h) {
            const float nmean = rl(mv, 31 + 32 * h), rinv = rl(rv, 31 + 32 * h);
            ROWDFT(a0[h], a1[h], nmean, rinv, xw + h * 14336);
            pst[4 * h] = nmean; pst[4 * h + 1] = rinv; pst[4 * h + 2] = rl(vs, 15 + 32 * h); pst[4 * h + 3] = rl(vs, 31 + 32 * h);
        }
        STAMP(20);
        BAR();
        const u32 tg = LR(u32, L_KEY + 96);
        const v4u q0 = {LQ(u32, xa0), LQ(u32, xa0 + 1792), LQ(u32, xa0 + 2 * 1792), LQ(u32, xa0 + 3 * 1792)};
        const v4u q1 = {LQ(u32, xa0 + 14336), LQ(u32, xa0 + 14336 + 1792), LQ(u32, xa0 + 14336 + 2 * 1792), LQ(u32, xa0 + 14336 + 3 * 1792)};
        const v4u q2 = {LQ(u32, xa2), LQ(u32, xa2 + 1792), LQ(u32, xa2 + 2 * 1792), LQ(u32, xa2 + 3 * 1792)};
#pragma unroll
        for (int h = 0; h < 2; ++h) { xs[2 * h] = LQ(v4u, xw + h * 14336); xs[2 * h + 1] = LQ(v4u, xw + h * 14336 + 80 - 16 * (g & 1) - 16 * g); }
        tag = __builtin_amdgcn_readfirstlane(tg);
        STAMP(3);
        const v4u r2a = w < 4 ? r8[0] : r8[2], r2b = w < 4 ? r8[1] : r8[3];
        const f4 dr0 = mh(q0, r8[0], z4), di0 = mh(q0, r8[1], z4), dr1 = mh(q1, r8[2], z4), di1 = mh(q1, r8[3], z4), dr2 = mh(q2, r2a, z4), di2 = mh(q2, r2b, z4);
        LQ(v4u, rsw) = v4u{pkh(dr0[0], di0[0]), pkh(dr0[1], di0[1]), pkh(dr0[2], di0[2]), tag};
        LQ(v4u, rsw + 8192) = v4u{pkh(dr1[0], di1[0]), pkh(dr1[1], di1[1]), pkh(dr1[2], di1[2]), tag};
        LQ(v4u, rsw + 16384) = v4u{pkh(dr2[0], di2[0]), pkh(dr2[1], di2[1]), pkh(dr2[2], di2[2]), tag};
        WS();
        const v4u v0 = LQ(v4u, rsr), v1 = LQ(v4u, rsr + 8192), v2 = LQ(v4u, rsr + 16384);
        const u32 e1s = WS_E1 + 2 * m * PITCH;
        ST16S(v0, e1l, e1s + 128 * w); ST16S(v1, e1l, e1s + PITCH + 128 * w); ST16S(v2, e1l, e1s + (w >> 2) * PITCH + 1024 + 128 * (w & 3));
        if ((w & 3) == 3) ST16S((v4u{pkh(dr2[3], di2[3]), 0u, 0u, tag}), l < 8 ? l * 16 : OOB, e1s + (w >> 2) * PITCH + 12288);
    }
    __builtin_amdgcn_sched_barrier(0);
    __builtin_amdgcn_s_setprio(0);
    STAMP(4);
    {
        u32 oa[4], da[4], s2r[3];
#pragma unroll
        for (int i = 0; i < 3; ++i) {
            const u32 c = t + 512 * i, k = (c * 43691u) >> 18, sub = c - 6 * k;
            const u32 al = (k & 3) | ((k >> 4) & 12), be = (k >> 2) & 15;
            oa[i] = (16 * al + be) * PITCH + 16 * sub; da[i] = lb + L_S1 + sub * 3888 + be * 80 + al * 4;
            s2r[i] = lb + L_S2 + sub * 4144 + k * 16;
            PIN(oa[i]); PIN(da[i]); PIN(s2r[i]);
        }
        const bool x4 = xtra && w < 4;
        oa[3] = x4 ? t * PITCH + 12288 : OOB; da[3] = lb + L_S1 + 18 * 1296 + (t & 15) * 80 + ((t >> 4) & 15) * 4;
        const u32 gso = WS_E1 + 96 * m, gsb = WS_E1 + 16 * (m >> 4), tb = x4 ? tag : 0u;
        PIN(oa[3]); PIN(da[3]);
#undef P2BAR
        u32 vx[2][8];
        SPEC2OP(vx[0], xs[0], xs[1]); SPEC2OP(vx[1], xs[2], xs[3]);
        const v4u lt3a = TABR(T_LT + 3);
#pragma unroll
        for (int i = 0; i < 8; ++i) { PIN(vx[0][i]); PIN(vx[1][i]); }
#define P2BAR PIN(g1a); PIN(g2a); PIN(g3b); PIN(cr); PIN(cn); \
        DRAIN(); \
        __builtin_amdgcn_s_sleep(DLY1); \
        STAMP(17); \
        v4u ga[4] = {LD16S(oa[0], gso), LD16S(oa[1], gso), LD16S(oa[2], gso), LD16S(oa[3], gsb)}; \
        asm volatile("s_barrier" ::: "memory"); STAMP(19)
        const int ua = 2 * w + ((m >> 4) & 1);
        P2PREP(ua, 1);
        u32 s2a = lt2.y + (ua / 3) * 4144 + (ua % 3) * 4, s2b = lt2.y + ((ua + 1) / 3) * 4144 + ((ua + 1) % 3) * 4;
        PIN(s1r); PIN(s2a); PIN(s2b);
        OP2LIN(vx[0], lt3a.z + w * 1168, lt3a.w + w * 1168); OP2LIN(vx[1], lt3a.z + w * 1168 + 9344, lt3a.w + w * 1168 + 9344);
        WS();
        PIN(ga[0]); PIN(ga[1]); PIN(ga[2]); PIN(ga[3]);
        _Pragma("nounroll") for (rt1 = 0; rt1 < SPIN; ++rt1) {
            if (__all((((ga[0].w ^ tag) | (ga[1].w ^ tag)) | ((ga[2].w ^ tag) | (ga[3].w ^ tb))) == 0)) break;
            WS();
            ga[0] = LD16S(oa[0], gso); ga[1] = LD16S(oa[1], gso); ga[2] = LD16S(oa[2], gso); ga[3] = LD16S(oa[3], gsb);
        }
        __builtin_amdgcn_s_setprio(PRIO);
#pragma unroll
        for (int i = 0; i < 3; ++i) { LQ(u32, da[i]) = ga[i].x; LQ(u32, da[i] + 1296) = ga[i].y; LQ(u32, da[i] + 2592) = ga[i].z; }
        if (x4) LQ(u32, da[3]) = ga[3].x;
        STAMP(28);
        BAR();
        f4 xr, xi, yr, yi;
        {
            const v4u a = LQ(v4u, s1r), b = LQ(v4u, s1r + 1296);
            STAMP(5);
            unit(a, fr[0], fi[0], xr, xi); unit(b, fr[1], fi[1], yr, yi);
        }
#pragma unroll
        for (int i = 0; i < 4; ++i) { LQ(u32, s2a + i * 256) = pkb(xr[i], xi[i]); LQ(u32, s2b + i * 256) = pkb(yr[i], yi[i]); }
        STAMP(26);
        BAR();
        v4u a = LQ(v4u, s2r[0]), b = LQ(v4u, s2r[1]), c = LQ(v4u, s2r[2]);
        STAMP(6);
        a.w = tag; b.w = tag; c.w = tag;
        const u32 e2s = WS_E2 + m * PITCH2 + w * 1024;
        ST16S(a, l16 - lb, e2s); ST16S(b, l16 - lb, e2s + 8192); ST16S(c, l16 - lb, e2s + 16384);
    }
    __builtin_amdgcn_s_setprio(0);
    STAMP(7);
    const v4u lt3 = TABR(T_LT + 3), lt4 = TABR(T_LT + 4);
    v4u r8a = TABR(T_R8I), r8b = TABR(T_R8I + 1), hc[8], hb0, hb1;
    f4 tw3[4];
    const bool sp = w == 0 && r == 12;
    u32 oa = sp ? (64 * (g >> 1) + 15) * PITCH2 + 24576 + 16 * (g & 1) - 160 * m : lt3.x, tb = (r < 12 || sp) ? tag : 0u;
    const u32 gso = WS_E2 + 192 * m + 2 * w * PITCH2;
    u32 yw_ = lt3.y + 144 * w, ya0 = lt3.z + w * 1168, ya1 = lt3.w + w * 1168, yw0 = lt4.x + rowo, yw1 = lt4.y + rowo, yr = lt4.z + rowo;
    PIN(r8a); PIN(r8b); PIN(oa); PIN(tb); PIN(yw_); PIN(ya0); PIN(ya1); PIN(yw0); PIN(yw1); PIN(yr);
    v4u xf[3], wf[3];
    u32 pw_ = lb + L_S1 + w * 256 + r * 16, pr_ = lb + L_S1 + g * 512 + r * 16;
    float cmw, caw;
    {
        const u32 xo_ = yw_ - (g == 3 ? 144 : 0), wd_ = L_TAB + T_RW * 1024 - L_Y - r * 1168;
#pragma unroll
        for (int i = 0; i < 3; ++i) { xf[i] = LQ(v4u, xo_ + 16 * i); wf[i] = LQ(v4u, xo_ + wd_ + 16 * i); }
        if (w == 0) {
            const u32 x8 = LQ(u32, xo_ - 48 * (g == 3 ? 0 : g) + 1152), w8 = LR(u32, L_TAB + T_RW * 1024 + 1152);
            xf[0].x = g == 3 ? x8 : xf[0].x; wf[0].x = g == 3 ? w8 : wf[0].x;
        }
        const f2 wc = LR(f2, L_KEY + 104);
        u32 rq[6];
#pragma unroll
        for (int i = 0; i < 3; ++i) { const v2u q = LQ(v2u, lt4.z + 16 * ROWP + 8 * i); rq[2 * i] = q.x; rq[2 * i + 1] = q.y; }
        float d[2];
#pragma unroll
        for (int h = 0; h < 2; ++h) {
            d[h] = 0.f;
#pragma unroll
            for (int i = 0; i < 3; ++i) { const v2u xx = LQ(v2u, yr + h * 8 * ROWP + 8 * i); d[h] = dot2(xx.x, rq[2 * i], d[h]); d[h] = dot2(xx.y, rq[2 * i + 1], d[h]); }
            d[h] = l < 48 ? d[h] : 0.f;
        }
        const float cxw = wsum2v(d[0], d[1]);
        const bool hi = l >= 32;
        if ((l & 31) == 31) {
            const u32 rc = lb + L_S1 + 2048 + (w + 8 * (l >> 5)) * 32;
            LQ(f4, rc) = f4{hi ? pst[6] : pst[2], hi ? pst[7] : pst[3], -2.f * (hi ? pst[4] : pst[0]), __builtin_amdgcn_rcpf((hi ? pst[5] : pst[1]) * (9.f * 288.f))};
            LQ(float, rc + 16) = cxw;
        }
        cmw = 576.f * wc.x; caw = wc.y;
#pragma unroll
        for (int i = 0; i < 3; ++i) { PIN(xf[i]); PIN(wf[i]); }
        PIN(cmw); PIN(caw); PIN(pw_); PIN(pr_);
    }
    DRAIN();
    __builtin_amdgcn_s_sleep(DLY2);
    STAMP(18);
    v4u ga[4];
#pragma unroll
    for (int i = 0; i < 4; ++i) ga[i] = LD16S(oa, gso + 16 * i * PITCH2);
    WS();
#pragma unroll
    for (int i = 0; i < 8; ++i) hc[i] = TABR(T_HC + i);
#pragma unroll
    for (int i = 0; i < 4; ++i) tw3[i] = TABF(T_TW3 + i);
    hb0 = TABR(T_HB); hb1 = TABR(T_HB + 1);
    v2u xq[8];
#pragma unroll
    for (int i = 0; i < 8; ++i) xq[i] = LQ(v2u, ((i & 1) ? yw1 : yw0) + (i >> 2) * 8 * ROWP + 32 * ((i >> 1) & 1));
    {
#pragma unroll
        for (int i = 0; i < 8; ++i) { PIN(xq[i]); PIN(hc[i]); }
        PIN(hb0); PIN(hb1);
#pragma unroll
        for (int i = 0; i < 4; ++i) { PIN(tw3[i]); PIN(ga[i]); }
        _Pragma("nounroll") for (rt2 = 0; rt2 < SPIN; ++rt2) {
            if (__all((((ga[0].w ^ tb) | (ga[1].w ^ tb)) | ((ga[2].w ^ tb) | (ga[3].w ^ tb))) == 0)) break;
            WS();
#pragma unroll
            for (int i = 0; i < 4; ++i) ga[i] = LD16S(oa, gso + 16 * i * PITCH2);
        }
        STAMP(29);
        __builtin_amdgcn_s_setprio(PRIO);
    }
    {
        STAMP(8);
        u32 y[12];
#pragma unroll
        for (int c = 0; c < 3; ++c) {
            const v4u a = {ga[0][c], ga[1][c], ga[2][c], ga[3][c]};
            const f4 dr = mb(a, r8a, z4), di = mb(a, r8b, z4);
#pragma unroll
            for (int i = 0; i < 4; ++i) y[3 * i + c] = pkb(dr[i], di[i]);
        }
        const v4u yf[3] = {v4u{y[0], y[1], y[2], y[3]}, v4u{y[4], y[5], y[6], y[7]}, v4u{y[8], y[9], y[10], y[11]}};
        v4u y0h = yf[0];
        if (w == 0) y0h.x = (g == 0 || g == 3) ? (pkb(blo(y[0]) * 0.5f, 0.f) & 0xFFFFu) : y[0];
        const f4 gy = mb(yf[2], yf[2], mb(yf[1], yf[1], mb(yf[0], y0h, z4)));
        const f4 gx = mb(yf[2], xf[2], mb(yf[1], xf[1], mb(yf[0], xf[0], z4)));
        const f4 gw = mb(yf[2], wf[2], mb(yf[1], wf[1], mb(yf[0], wf[0], z4)));
        if (g < 3) {
#pragma unroll
            for (int i = 0; i < 3; ++i) LQ(v4u, yw_ + 16 * i) = yf[i];
        } else if (w == 0) {
            LQ(u32, yw_ + 1152 - 144) = y[0];
        }
        {
            const bool b0 = r & 1, b1 = r & 2;
            const f4 p = {b1 ? (b0 ? gy[3] : gy[2]) : (b0 ? gy[1] : gy[0]), b1 ? (b0 ? gx[3] : gx[2]) : (b0 ? gx[1] : gx[0]), b1 ? (b0 ? gw[3] : gw[2]) : (b0 ? gw[1] : gw[0]), 0.f};
            if (g == (r >> 2)) LQ(f4, pw_) = p;
            WS();
            if (w == 0 && g == 0) LQ(float, pw_ + 12) = blo(y[0]);
        }
        if (w == 7) ST16((v4u{(u32)__builtin_amdgcn_s_memrealtime(), tag, tag, tag}), l == 0 ? WS_HINT + m * 128 : OOB);
        STAMP(27);
    }
    {
        BAR();
        f4 ps = LQ(f4, pr_) + LQ(f4, pr_ + 256);
        const f4 rc = LQ(f4, lb + L_S1 + 2048 + r * 32);
        const float cxw = LQ(float, lb + L_S1 + 2048 + r * 32 + 16);
        v4u A0[2], A1[2];
#pragma unroll
        for (int h = 0; h < 2; ++h) {
            A0[h] = v4u{LQ(u32, ya0 + h * 9344), LQ(u32, ya0 + h * 9344 + 96), LQ(u32, ya0 + h * 9344 + 192), LQ(u32, ya0 + h * 9344 + 288)};
            A1[h] = v4u{LQ(u32, ya1 + h * 9344), LQ(u32, ya1 + h * 9344 + 96), LQ(u32, ya1 + h * 9344 + 192), LQ(u32, ya1 + h * 9344 + 288)};
        }
        STAMP(9);
#pragma unroll
        for (int i = 0; i < 4; ++i) {
            const auto q = __builtin_amdgcn_permlane16_swap(BC(u32, swadd32(ps[i], ps[i])), BC(u32, swadd32(ps[i], ps[i])), false, false);
            const u32 q0 = q[0], q1 = q[1];
            ps[i] = BC(float, q0) + BC(float, q1);
        }
        const float ey = ps[3] * (1.f / 18.f);
        const float m1 = rc[0] + ey, e2 = rc[1] + ps[1] * rc[3] + rc[2] * ey + ps[0] * (1.f / 162.f);
        const float rv = __builtin_amdgcn_rcpf(rbf(__builtin_amdgcn_sqrtf(rbf(rbf(fmaxf(e2 - m1 * m1, 0.f)) + 1e-5f)))), mv = rbf(m1);
        const float rs_ = (cxw + ps[2] * caw + cmw * (ey - mv)) * rv;
        const u32 rbits = pkb(rs_, rs_) & 0xFFFFu;
        const u32 srt = (rbits & 0x7FFFu) > 0x7F80u ? 0u : ((rbits & 0x8000u) ? (~rbits & 0xFFFFu) : (rbits | 0x8000u));
        const u32 key = (srt << 16) | ((2047 - 2 * m - 256 * (r & 7) - (r >> 3)) << 1);
        u32 k1 = key;
        k1 = umax(k1, dppu<0x128, 0xf>(k1)); k1 = umax(k1, dppu<0x124, 0xf>(k1)); k1 = umax(k1, dppu<0x122, 0xf>(k1)); k1 = umax(k1, dppu<0x121, 0xf>(k1));
        u32 k2 = key == k1 ? 0u : key;
        k2 = umax(k2, dppu<0x128, 0xf>(k2)); k2 = umax(k2, dppu<0x124, 0xf>(k2)); k2 = umax(k2, dppu<0x122, 0xf>(k2)); k2 = umax(k2, dppu<0x121, 0xf>(k2));
        if (w == 0) ST16((v4u{k1, k2 | 1u, tag, tag}), l == 0 ? WS_E3 + m * 128 : OOB);
        STAMP(10);
        k1 = __builtin_amdgcn_readfirstlane(k1); k2 = __builtin_amdgcn_readfirstlane(k2);
        u32 myk[2] = {(u32)__builtin_amdgcn_readlane(key, w), (u32)__builtin_amdgcn_readlane(key, w + 8)};
        const bool c0 = myk[0] == k1 || myk[0] == k2, c1 = myk[1] == k1 || myk[1] == k2, cand = c0 || c1;
        if (cand) __builtin_amdgcn_s_setprio(3); else __builtin_amdgcn_s_setprio(0);
        const int hs = c1 && !c0;
        if (hs) {
            const v4u t0 = A0[0], t1 = A1[0]; A0[0] = A0[1]; A1[0] = A1[1]; A0[1] = t0; A1[1] = t1;
#pragma unroll
            for (int i = 0; i < 4; ++i) { const v2u tq = xq[i]; xq[i] = xq[4 + i]; xq[4 + i] = tq; }
        }
#pragma unroll
        for (int it = 0; it < 2; ++it) {
            const int h = it ^ hs;
            const u32 ro_ = h * 8 * ROWP;
#pragma unroll
            for (int bt = 0; bt < 2; ++bt) {
                const v4u f = rot<true>(mb(A1[it], hc[4 + 2 * bt], mb(A0[it], hc[2 * bt], z4)), mb(A1[it], hc[5 + 2 * bt], mb(A0[it], hc[2 * bt + 1], z4)), tw3[2 * bt], tw3[2 * bt + 1]);
                const v2u q0 = xq[4 * it + 2 * bt], q1 = xq[4 * it + 2 * bt + 1];
                const f4 y0 = mb(f, hb0, f4{blo(q0.x), bhi(q0.x), blo(q0.y), bhi(q0.y)}), y1 = mb(f, hb1, f4{blo(q1.x), bhi(q1.x), blo(q1.y), bhi(q1.y)});
                if (bt == 0 || g < 2) {
                    LQ(v2u, yw0 + ro_ + 32 * bt) = v2u{pkb(y0[0], y0[1]), pkb(y0[2], y0[3])};
                    LQ(v2u, yw1 + ro_ + 32 * bt) = v2u{pkb(y1[0], y1[1]), pkb(y1[2], y1[3])};
                }
            }
            WS();
            u32 hp[6], lp[6];
#pragma unroll
            for (int i = 0; i < 3; ++i) { const v2u y = LQ(v2u, yr + ro_ + 8 * i); hp[2 * i] = y.x; hp[2 * i + 1] = y.y; }
            const float mean = rl(mv, w + 8 * h), rinv = rl(rv, w + 8 * h);
#pragma unroll
            for (int i = 0; i < 6; ++i) lp[i] = pkbo((unp(hp[i]) - mean) * rinv);
            if (h ? c1 : c0) {
                const int rank = (h ? myk[1] : myk[0]) == k2;
                const u32 cs = L_CS + rank * 3200, cw = cs + (l < 48 ? l * 32 : 3072);
                LR(v4u, cw) = v4u{lp[0], lp[1], lp[2], tag}; LR(v4u, cw + 16) = v4u{lp[3], lp[4], lp[5], tag};
                if (l < 48) { LR(v4u, cw + 1536) = v4u{hp[0], hp[1], hp[2], tag}; LR(v4u, cw + 1552) = v4u{hp[3], hp[4], hp[5], tag}; }
                WS();
#pragma unroll
                for (int i = 0; i < 3; ++i) { const v4u c = LQ(v4u, l16 + cs + 1024 * i); ST16S(c, l16 - lb + 1024 * i, WS_CAND + m * 6144 + rank * 3072); }
            }
            if (l < 48) {
#pragma unroll
                for (int i = 0; i < 3; ++i) LQ(v2u, yr + ro_ + 8 * i) = v2u{pkbo(unp(hp[2 * i]) + unp(lp[2 * i])), pkbo(unp(hp[2 * i + 1]) + unp(lp[2 * i + 1]))};
            }
            WS();
            __builtin_amdgcn_sched_barrier(0);
        }
        u32 K1 = 0, K2 = 0;
        if (cand) {
            __builtin_amdgcn_s_sleep(24);
            TOP2(K1, K2, );
            K1 &= ~1u; K2 &= ~1u;
        }
#pragma unroll
        for (int h = 0; h < 2; ++h) {
            if (cand && (myk[h] == K1 || myk[h] == K2)) continue;
            const v4u v = LQ(v4u, l16 + L_ROW + rowo + h * 8 * ROWP), v2 = LR(v4u, L_ROW + rowo + h * 8 * ROWP + 1024 + (l & 7) * 16);
            ST16R(ro, v, l * 16, (row + h) * 1152, OAUX);
            ST16R(ro, v2, l < 8 ? l * 16 : OOB, (row + h) * 1152 + 1024, OAUX);
        }
    }
    SDUMP(0, 10, 0); SDUMP(17, 22, 0); SDUMP(24, 24, 0);
    WDUMP();
}

extern "C" void kernel_launch(void* const* d_in, const int* in_sizes, int n_in, void* d_out, int out_size, void* d_ws, size_t ws_size,
                              hipStream_t stream) {
    fused_block<<<256, 768, 0, stream>>>((const u16*)d_in[0], (const u16*)d_in[1], (const u16*)d_in[2], (const u16*)d_in[4],
                                         (const u16*)d_in[5], (const u16*)d_in[6], (const u16*)d_in[7], (u16*)d_out, (unsigned char*)d_ws);
}
```
